```python
import torch

def _softmax(x, axis=-1):
    return torch.softmax(x, dim=axis)

def _gelu(x):
    return torch.nn.functional.gelu(x, approximate='tanh')

def _silu(x):
    return torch.nn.functional.silu(x)

def _sigmoid(x):
    return torch.sigmoid(x)

def _layernorm(x, axis=-1, eps=1e-05):
    m = x.mean(dim=axis, keepdim=True)
    v = x.var(dim=axis, keepdim=True, unbiased=False)
    return (x - m) / torch.sqrt(v + eps)

def _scatter_add(tgt, idx, src, axis=0):
    return tgt.clone().index_add_(axis, idx, src)

def _segment_sum(src, seg, n):
    out = torch.zeros((n,) + src.shape[1:], dtype=src.dtype)
    return out.index_add_(0, seg, src)

def _pad(x, pw):
    flat = tuple((v for pair in reversed(pw) for v in pair))
    return torch.nn.functional.pad(x, flat)
kernel_name = 'gated_mix__compressed_kv_cla__mixer_gmlp__prompt'

def setup_inputs():

    _w = lambda a: (lambda t: t.to(torch.bfloat16) if t.is_floating_point() else t)(torch.as_tensor(a))

    def _A_setup():
        B, H, Sq, Sk, D, M = (1, 32, 2048, 2048, 128, 512)
        Dc = 4 * D
        return {'x': _w((lambda *s: torch.randn(*s, dtype=torch.bfloat16) * 0.05)(B, Sq, M)), 'c_kv': _w((lambda *s: torch.randn(*s, dtype=torch.bfloat16) * 0.8)(B, Sk, Dc)), 'w_q': _w((lambda *s: torch.randn(*s, dtype=torch.bfloat16) * 6.4)(M, H, D) * M ** (-0.5)), 'w_uk': _w((lambda *s: torch.randn(*s, dtype=torch.bfloat16) * 6.4)(Dc, H, D) * Dc ** (-0.5)), 'w_uv': _w((lambda *s: torch.randn(*s, dtype=torch.bfloat16) * 0.8)(Dc, H, D) * Dc ** (-0.5)), 'w_o': _w((lambda *s: torch.randn(*s, dtype=torch.bfloat16) * 0.8)(H, D, M) * (H * D) ** (-0.5))}
    _A_in = _A_setup()

    def _B_setup():
        B, S, D, F = (1, 2048, 512, 1792)
        Fh = F // 2
        return {'x': _w((lambda *s: torch.randn(*s, dtype=torch.bfloat16) * 0.05)(B, S, D)), 'w_in': _w((lambda *s: torch.randn(*s, dtype=torch.bfloat16) * 0.4)(D, 2 * Fh)), 'w_sp': _w(0.01 * (lambda *s: torch.randn(*s, dtype=torch.bfloat16) * 0.4)(S, S)), 'b_sp': _w(1.0 + 0.01 * (lambda *s: torch.randn(*s, dtype=torch.bfloat16) * 0.05)(S)), 'w_out': _w((lambda *s: torch.randn(*s, dtype=torch.bfloat16) * 0.05)(Fh, D))}
    _B_in = _B_setup()
    return {'A_x': _A_in['x'], 'A_c_kv': _A_in['c_kv'], 'A_w_q': _A_in['w_q'], 'A_w_uk': _A_in['w_uk'], 'A_w_uv': _A_in['w_uv'], 'A_w_o': _A_in['w_o'], 'B_x': _B_in['x'], 'B_w_in': _B_in['w_in'], 'B_w_sp': _B_in['w_sp'], 'B_b_sp': _B_in['b_sp'], 'B_w_out': _B_in['w_out'], 'gate_w': _w((lambda *s: torch.randn(*s, dtype=torch.bfloat16) * 0.05)(512) * 16.0)}

def reference(A_x, A_c_kv, A_w_q, A_w_uk, A_w_uv, A_w_o, B_x, B_w_in, B_w_sp, B_b_sp, B_w_out, gate_w):

    def _A_ref(x, c_kv, w_q, w_uk, w_uv, w_o):
        B, Sq, M = (x.shape[0], x.shape[1], x.shape[2])
        Sk = c_kv.shape[1]
        H, D = (w_q.shape[1], w_q.shape[2])
        q = torch.einsum('bqm,mhd->bhqd', x, w_q)
        k = torch.einsum('bkc,chd->bhkd', c_kv, w_uk)
        v = torch.einsum('bkc,chd->bhkd', c_kv, w_uv)
        scores = torch.einsum('bhqd,bhkd->bhqk', q, k) * D ** (-0.5)
        off = Sk - Sq
        qi = torch.arange(Sq)[:, None]
        kj = torch.arange(Sk)[None, :]
        cmask = kj <= qi + off
        scores = torch.where(cmask[None, None], scores, -1000000000.0)
        p = _softmax(scores, axis=-1)
        ctx = torch.einsum('bhqk,bhkd->bhqd', p, v)
        return torch.einsum('bhqd,hdm->bqm', ctx, w_o)

    def _B_ref(x, w_in, w_sp, b_sp, w_out):
        B, S, D = x.shape
        F = w_in.shape[-1]
        Fh = F // 2
        z = _gelu(torch.einsum('bsd,df->bsf', x, w_in))
        z1 = z[..., :Fh].clone()
        z2 = _layernorm(z[..., Fh:], axis=-1)
        g = torch.einsum('ts,bsf->btf', w_sp, z2) + b_sp[None, :, None]
        return torch.einsum('bsf,fd->bsd', z1 * g, w_out)
    x = A_x
    g = _sigmoid((x * gate_w).sum(-1, keepdims=True))
    a = _A_ref(x, A_c_kv, A_w_q, A_w_uk, A_w_uv, A_w_o)
    b = _B_ref(x, B_w_in, B_w_sp, B_b_sp, B_w_out)
    return g * a + (1 - g) * b
```

```cpp
#include <hip/hip_runtime.h>
#include <hip/hip_bf16.h>
#include <utility>
#pragma clang diagnostic ignored "-Winline-asm"
typedef unsigned short u16;
typedef short bf16x8 __attribute__((ext_vector_type(8)));
typedef float f32x4 __attribute__((ext_vector_type(4)));
typedef float f32x2 __attribute__((ext_vector_type(2)));
typedef __bf16 bf16x2_t __attribute__((ext_vector_type(2)));
typedef unsigned u32x4 __attribute__((ext_vector_type(4)));
typedef __amdgpu_buffer_rsrc_t srd_t;
#define LAS __attribute__((address_space(3)))
#define DI __device__ __forceinline__
#define SBAR() __builtin_amdgcn_sched_barrier(0)
#define PIN(x) asm volatile("" : "+v"(x))
#define AI __attribute__((always_inline))
template<int... Is,class F> DI void sfor_(std::integer_sequence<int,Is...>, F f){ (f(std::integral_constant<int,Is>{}), ...); }
template<int N,class F> DI void sfor(F f){ sfor_(std::make_integer_sequence<int,N>{}, f); }
typedef const LAS char* ldsp;
typedef LAS char* ldsw;

constexpr unsigned WS_FLG  = 0;
constexpr unsigned WS_KV   = 32768;
constexpr unsigned WS_AF   = WS_KV + 32u*32u*32768u;
constexpr unsigned WS_H    = WS_AF + 128u*128u*1024u;
constexpr unsigned WS_Z1   = WS_H + 128u*56u*1024u;
constexpr unsigned WS_Z2   = WS_Z1 + 64u*56u*1024u;
constexpr unsigned WS_ST   = WS_Z2 + 28u*128u*1024u;
constexpr unsigned WS_GATE = WS_ST + 2048u*256u;
constexpr unsigned WS_PART = WS_GATE + 8192u;
constexpr unsigned WS_END  = WS_PART + 64u*16u*16384u;

template<bool Z=false> DI void mm2(f32x4& acc, bf16x8 a0, bf16x8 b0, bf16x8 a1, bf16x8 b1){
  if(Z) asm volatile("v_mfma_f32_16x16x32_bf16 %0, %1, %2, 0\n\tv_mfma_f32_16x16x32_bf16 %0, %3, %4, %0" : "=&v"(acc) : "v"(a0), "v"(b0), "v"(a1), "v"(b1));
  else  asm volatile("v_mfma_f32_16x16x32_bf16 %0, %1, %2, %0\n\tv_mfma_f32_16x16x32_bf16 %0, %3, %4, %0" : "+v"(acc) : "v"(a0), "v"(b0), "v"(a1), "v"(b1)); }
DI void mm2c(f32x4& acc, f32x4 c, bf16x8 a0, bf16x8 b0, bf16x8 a1, bf16x8 b1){
  asm volatile("v_mfma_f32_16x16x32_bf16 %0, %1, %2, %5\n\tv_mfma_f32_16x16x32_bf16 %0, %3, %4, %0" : "=&v"(acc) : "v"(a0), "v"(b0), "v"(a1), "v"(b1), "v"(c)); }
#define MFENCE() do{ asm volatile("s_nop 7\n\ts_nop 3"); SBAR(); }while(0)
DI unsigned cvtpk(float lo, float hi){ f32x2 v={lo,hi}; bf16x2_t b=__builtin_convertvector(v,bf16x2_t); return __builtin_bit_cast(unsigned,b); }
DI int otid(){ int t=threadIdx.x; asm volatile("" : "+v"(t)); return t; }
DI unsigned rfl(unsigned x){ return __builtin_amdgcn_readfirstlane(x); }
DI float bflo(unsigned w){ return __uint_as_float(w<<16); }
DI float bfhi(unsigned w){ return __uint_as_float(w&0xffff0000u); }
DI srd_t mksrd(const void* p, unsigned bytes){ return __builtin_amdgcn_make_buffer_rsrc((void*)p,(short)0,(int)bytes,0x00020000); }
DI void setm0(unsigned v){ asm volatile("s_mov_b32 m0, %0" :: "s"(v) : "memory"); }
template<int OFF> DI void dma1(const void* gbase, unsigned voff){
  asm volatile("global_load_lds_dwordx4 %0, %1 offset:%2 sc1" :: "v"(voff), "s"(gbase), "i"(OFF) : "memory");
}
DI void dma4(const void* gbase, unsigned voff, unsigned ldsb){
  asm volatile("s_mov_b32 m0, %2\n\ts_nop 0\n\t"
               "global_load_lds_dwordx4 %0, %1 offset:0 sc1\n\t"
               "global_load_lds_dwordx4 %0, %1 offset:1024 sc1\n\t"
               "global_load_lds_dwordx4 %0, %1 offset:2048 sc1\n\t"
               "global_load_lds_dwordx4 %0, %1 offset:3072 sc1" :: "v"(voff), "s"(gbase), "s"(ldsb) : "memory");
}
DI void bdmai(unsigned ldsb, int loff, unsigned voff, srd_t srd, unsigned sbase, int soff){
  unsigned tmp;
  asm volatile("s_add_i32 m0, %1, %5\n\ts_add_i32 %0, %4, %6\n\tbuffer_load_dwordx4 %2, %3, %0 offen lds" : "=&s"(tmp) : "s"(ldsb), "v"(voff), "s"(srd), "s"(sbase), "i"(loff), "i"(soff) : "memory", "scc");
}
DI void bdmas(unsigned ldsb, int loff, unsigned voff, srd_t srd, unsigned sbase, int soff){
  unsigned tmp;
  asm volatile("s_add_i32 m0, %1, %5\n\ts_add_i32 %0, %4, %6\n\tbuffer_load_dwordx4 %2, %3, %0 offen sc1 lds" : "=&s"(tmp) : "s"(ldsb), "v"(voff), "s"(srd), "s"(sbase), "i"(loff), "i"(soff) : "memory", "scc");
}
#define WAITV(n) asm volatile("s_waitcnt vmcnt(" #n ")" ::: "memory")
template<int N> DI void waitv_bar(){ asm volatile("s_waitcnt vmcnt(%0) lgkmcnt(0)\n\ts_barrier" :: "i"(N) : "memory"); }
#define BARRIER() asm volatile("s_waitcnt lgkmcnt(0)\n\ts_barrier" ::: "memory")
#define BARRIER_NW() asm volatile("s_barrier" ::: "memory")
#define LDF(p,off) (*(const LAS bf16x8*)((p)+(off)))
typedef short v4i16_t __attribute__((ext_vector_type(4)));
DI bf16x8 LDT(ldsp p, int off){
  v4i16_t lo=__builtin_amdgcn_ds_read_tr16_b64_v4i16((LAS v4i16_t*)(p+off)), hi=__builtin_amdgcn_ds_read_tr16_b64_v4i16((LAS v4i16_t*)(p+off+1024));
  return __builtin_shufflevector(lo,hi,0,1,2,3,4,5,6,7);
}
#define MX3(a,b,c) __builtin_fmaxf(__builtin_fmaxf((a),(b)),(c))
DI void mul4(f32x4& v,float f){ float a=v[0],b=v[1],c=v[2],d=v[3]; asm("v_mul_f32_e32 %0, %0, %4\n\tv_mul_f32_e32 %1, %1, %4\n\tv_mul_f32_e32 %2, %2, %4\n\tv_mul_f32_e32 %3, %3, %4":"+v"(a),"+v"(b),"+v"(c),"+v"(d):"v"(f)); v=(f32x4){a,b,c,d}; }
DI void sub4(f32x4& v,float f){ float a=v[0],b=v[1],c=v[2],d=v[3]; asm("v_sub_f32_e32 %0, %0, %4\n\tv_sub_f32_e32 %1, %1, %4\n\tv_sub_f32_e32 %2, %2, %4\n\tv_sub_f32_e32 %3, %3, %4":"+v"(a),"+v"(b),"+v"(c),"+v"(d):"v"(f)); v=(f32x4){a,b,c,d}; }
DI float fmov_s(float a){ float r; asm("v_mov_b32_e32 %0, %1":"=v"(r):"v"(a)); return r; }
DI float xhalf_max(float v){ auto rr=__builtin_amdgcn_permlane32_swap(__float_as_uint(v),__float_as_uint(v),false,false); return __builtin_fmaxf(__uint_as_float(rr[0]),__uint_as_float(rr[1])); }
DI float xhalf_sum(float v){ auto rr=__builtin_amdgcn_permlane32_swap(__float_as_uint(v),__float_as_uint(v),false,false); return __uint_as_float(rr[0])+__uint_as_float(rr[1]); }
DI f32x2 gelu2(f32x2 x){
  constexpr float K0=-2.f*0.7978845608028654f*1.4426950408889634f, K1=K0*0.044715f;
  f32x2 k0={K0,K0}, k1={K1,K1};
  f32x2 t=__builtin_elementwise_fma(x*x,k1,k0)*x;
  t[0]=__builtin_amdgcn_exp2f(t[0]); t[1]=__builtin_amdgcn_exp2f(t[1]);
  t+=(f32x2){1.f,1.f};
  t[0]=__builtin_amdgcn_rcpf(t[0]); t[1]=__builtin_amdgcn_rcpf(t[1]);
  return x*t; }

typedef unsigned long long u64;
typedef __attribute__((address_space(1))) u64 gu64;
typedef unsigned u32x2 __attribute__((ext_vector_type(2)));
DI void st16(srd_t w, unsigned voff, unsigned soff, u32x4 v){ __builtin_amdgcn_raw_buffer_store_b128(v,w,voff+soff,0,16); }
DI void st4(srd_t w, unsigned voff, unsigned soff, unsigned v){ __builtin_amdgcn_raw_buffer_store_b32(v,w,voff+soff,0,16); }
DI u32x4 ld16(srd_t w, unsigned voff, unsigned soff){ return __builtin_amdgcn_raw_buffer_load_b128(w,voff,soff,16); }
DI void poll_issue(char* ws, unsigned fa){ asm volatile("global_load_dwordx2 v[252:253], %0, %1 sc1" :: "v"(fa), "s"(ws+WS_FLG) : "memory"); }
DI void poll_finish(char* ws, unsigned fa, bool active, u64 tag){
  unsigned lo,hi; asm volatile("s_waitcnt vmcnt(0)\n\tv_mov_b32 %0, v252\n\tv_mov_b32 %1, v253" : "=v"(lo), "=v"(hi) :: "memory");
  u64 v=((u64)hi<<32)|lo;
#pragma nounroll
  for(unsigned spin=0;spin<(1u<<16);++spin){
    if(__builtin_amdgcn_ballot_w64(active && v!=tag)==0) break;
    __builtin_amdgcn_s_sleep(1);
    asm volatile("global_load_dwordx2 %0, %1, %2 sc1\n\ts_waitcnt vmcnt(0)" : "=&v"(v) : "v"(fa), "s"(ws+WS_FLG) : "memory");
  }
  __builtin_amdgcn_fence(__ATOMIC_ACQUIRE,"wavefront");
}
DI void poll_check(char* ws, unsigned fa, bool active, u64 tag){
  unsigned lo,hi; asm volatile("v_mov_b32 %0, v252\n\tv_mov_b32 %1, v253" : "=v"(lo), "=v"(hi) :: "memory");
  u64 v=((u64)hi<<32)|lo;
#pragma nounroll
  for(unsigned spin=0;spin<(1u<<16);++spin){
    if(__builtin_amdgcn_ballot_w64(active && v!=tag)==0) break;
    __builtin_amdgcn_s_sleep(1);
    asm volatile("global_load_dwordx2 %0, %1, %2 sc1\n\ts_waitcnt vmcnt(0)" : "=&v"(v) : "v"(fa), "s"(ws+WS_FLG) : "memory");
  }
  __builtin_amdgcn_fence(__ATOMIC_ACQUIRE,"wavefront");
}
template<int R> DI void mb_ld16(srd_t r, unsigned off){ asm volatile("buffer_load_dwordx4 v[%c2:%c3], %0, %1, 0 offen sc1" :: "v"(off), "s"(r), "i"(R), "i"(R+3) : "memory"); }
template<int R> DI void mb_ld4(srd_t r, unsigned off){ asm volatile("buffer_load_dword v%c2, %0, %1, 0 offen sc1" :: "v"(off), "s"(r), "i"(R) : "memory"); }
template<int R> DI void mb_ld2(srd_t r, unsigned off){ asm volatile("buffer_load_ushort v%c2, %0, %1, 0 offen" :: "v"(off), "s"(r), "i"(R) : "memory"); }
template<int R> DI unsigned mb_get(){ unsigned d; asm volatile("v_mov_b32 %0, v%c1" : "=v"(d) : "i"(R) : "memory"); return d; }

DI unsigned a_src(int lane,int par,unsigned stride){ const int r=lane>>3; return r*stride + (((lane&7) ^ ((4*par+(r>>1))&7))<<4); }
DI unsigned b_src(int lane,unsigned stride){ const int r=lane>>4; return r*stride + (((lane&15) ^ (r<<2))<<4); }

struct GS { f32x4 acc[12][2]; bf16x8 X[2][2][2], W[3][2]; ldsp xp[2]; unsigned wp, ip; };
DI void padd(ldsp& p,int d){ p+=d; asm volatile("" : "+v"(p)); }
DI void uadd(unsigned& p,int d){ p+=d; asm volatile("" : "+v"(p)); }
DI bf16x8 LDT2(unsigned a,int off,int hi){ ldsp p=(ldsp)(size_t)a;
  v4i16_t l=__builtin_amdgcn_ds_read_tr16_b64_v4i16((LAS v4i16_t*)(p+off)), h=__builtin_amdgcn_ds_read_tr16_b64_v4i16((LAS v4i16_t*)(p+off+hi));
  return __builtin_shufflevector(l,h,0,1,2,3,4,5,6,7); }
DI u32x4 pack2(f32x4 lo,f32x4 hi){ return (u32x4){cvtpk(lo[0],lo[1]),cvtpk(lo[2],lo[3]),cvtpk(hi[0],hi[1]),cvtpk(hi[2],hi[3])}; }
DI void gs_init(GS& G, char* lds, int arow0){
  const int lane=otid()&63, c=lane&15, g=lane>>4, q=c>>2, p=c&3; const unsigned l0=(unsigned)(size_t)lds;
#pragma unroll
  for(int s=0;s<2;++s){ G.xp[s]=(ldsp)lds+(arow0+c)*128+(((4*s+g)^(c>>1))<<4); padd(G.xp[s],0); }
  G.wp=l0+32768+(8*g+q)*256+(((p>>1)^((q<<2)|((g&1)<<1)))<<4)+(p&1)*8; uadd(G.wp,0);
  G.ip=l0+49152+(8*g+q)*64+(g&1)*32+p*8; uadd(G.ip,0);
}
DI void gs_ldw(GS& G,int cb){
  if(cb<8){ const unsigned a=G.wp^(cb<<5); G.W[cb%3][0]=LDT2(a,0,1024); G.W[cb%3][1]=LDT2(a,8192,1024); }
  else { const unsigned a=G.ip^((cb&1)<<5); const int o=((cb-8)>>1)*4096; G.W[cb%3][0]=LDT2(a,o,256); G.W[cb%3][1]=LDT2(a,o+2048,256); }
}
template<int T,class F> DI void ktile(GS& G,int d,int db,int nops,int span,F op){
  constexpr int N0=T==0?4:0, N1=T==7?28:24;
  if(T==0){ gs_ldw(G,0);
#pragma unroll
    for(int i=0;i<4;++i) G.X[0][i>>1][i&1]=LDF(G.xp[i&1],(i>>1)*2048);
    gs_ldw(G,1); }
  SBAR();
#pragma unroll
  for(int n=N0;n<N1;++n){ const int cb=((n>>1)+10)%12, rb=n&1, xb= n<4? (T+1)&1 : T&1;
    const bool Z= T==0 || (T==1 && n<4);
    if(cb<10){ if(Z) mm2<true>(G.acc[cb][rb],G.W[cb%3][0],G.X[xb][rb][0],G.W[cb%3][1],G.X[xb][rb][1]); else mm2(G.acc[cb][rb],G.W[cb%3][0],G.X[xb][rb][0],G.W[cb%3][1],G.X[xb][rb][1]); }
    else     { if(Z) mm2<true>(G.acc[cb][rb],G.X[xb][rb][0],G.W[cb%3][0],G.X[xb][rb][1],G.W[cb%3][1]); else mm2(G.acc[cb][rb],G.X[xb][rb][0],G.W[cb%3][0],G.X[xb][rb][1],G.W[cb%3][1]); }
    if(rb==0 && n<24) gs_ldw(G,n>>1);
    if(n<2){ G.X[T&1][n][0]=LDF(G.xp[0],n*2048); G.X[T&1][n][1]=LDF(G.xp[1],n*2048); }
    if(n==23 && d!=0){ padd(G.xp[0],d); padd(G.xp[1],d); uadd(G.wp,db); uadd(G.ip,db); }
#pragma unroll
    for(int i2=0;i2<24;++i2) if(i2<nops && (i2*(span*6<N1-N0?span*6:N1-N0))/nops==n-N0) op(i2);
    SBAR();
  }
}
template<int N> DI void waitv_barn(){ asm volatile("s_waitcnt vmcnt(%0)\n\ts_barrier" :: "i"(N) : "memory"); }
template<int T=0> DI void flag_set(srd_t sw,int f,int idx,u64 tag){ if(rfl(threadIdx.x>>6)==T/64){ const int l=otid()&63; asm volatile("" : "+s"(tag));
  __builtin_amdgcn_raw_buffer_store_b64((u32x2){(unsigned)tag,(unsigned)(tag>>32)},sw,l==0? WS_FLG+f*4096+idx*8 : 0xF0000000u,0,16); } }
DI void touch_asm(srd_t r, unsigned off){ asm volatile("buffer_load_dword v255, %0, %1, 0 offen sc1" :: "v"(off), "s"(r) : "memory"); }

template<int NL> DI void code_touch(int slot){ u64 pc; unsigned sz; asm volatile("s_getpc_b64 %0\n1:\n\ts_mov_b32 %1, Lcode_end-1b" : "=s"(pc), "=s"(sz));
  const int l=otid()&63; touch_asm(mksrd((const void*)pc,sz),l<NL? (slot*NL+l)*128 : 0xF0000000u); }
template<int CTRL> DI float dpp_add(float v){ return v+__uint_as_float(__builtin_amdgcn_update_dpp(0,__float_as_uint(v),CTRL,0xf,0xf,true)); }
DI float red32(float v){
  v=dpp_add<0x121>(v); v=dpp_add<0x122>(v); v=dpp_add<0x124>(v); v=dpp_add<0x128>(v);
  auto rr=__builtin_amdgcn_permlane16_swap(__float_as_uint(v),__float_as_uint(v),false,false);
  return __uint_as_float(rr[0])+__uint_as_float(rr[1]);
}
DI float x16_sum(float v){ auto rr=__builtin_amdgcn_permlane16_swap(__float_as_uint(v),__float_as_uint(v),false,false); return __uint_as_float(rr[0])+__uint_as_float(rr[1]); }
DI float x16_max(float v){ auto rr=__builtin_amdgcn_permlane16_swap(__float_as_uint(v),__float_as_uint(v),false,false); return __builtin_fmaxf(__uint_as_float(rr[0]),__uint_as_float(rr[1])); }
DI float fma_s(float a,float b,float c){ float r; asm("v_fma_f32 %0, %1, %2, %3":"=v"(r):"v"(a),"v"(b),"v"(c)); return r; }
DI void ln_stats(float (&st)[4]){
  const bool odd=otid()&1;
#pragma unroll
  for(int k=0;k<4;++k){ float v=x16_sum(st[k]); PIN(v);
    v=xhalf_sum(v)*(1.f/896.f); PIN(v);
    const float o=__uint_as_float(__builtin_amdgcn_update_dpp(0,__float_as_uint(v),0xB1,0xf,0xf,true));
    const float mean=odd? o : v, ms=odd? v : o;
    float rs=__builtin_amdgcn_rsqf(__builtin_fmaxf(__builtin_fmaf(-mean,mean,ms),0.f)+1e-5f); PIN(rs);
    st[k]=odd? -mean*rs : rs; PIN(st[k]); }
}
DI void ln_apply(u32x4 (&zr)[2], ldsp sp){
  const int l=otid()&63; sp+=((l>>4)&1)*128+(l>>5)*64;
#pragma unroll
  for(int u=0;u<2;++u){ const f32x4 a=*(const LAS f32x4*)(sp+u*32), c=*(const LAS f32x4*)(sp+u*32+16);
#pragma unroll
    for(int s=0;s<2;++s)
#pragma unroll
      for(int q=0;q<2;++q) zr[s][2*u+q]=cvtpk(fma_s(bflo(zr[s][2*u+q]),a[2*q],c[2*q]),fma_s(bfhi(zr[s][2*u+q]),a[2*q+1],c[2*q+1])); }
}
template<int CTRL,int BM> DI float dpp_sel(float old,float v){ return __uint_as_float(__builtin_amdgcn_update_dpp(__float_as_uint(old),__float_as_uint(v),CTRL,0xf,BM,false)); }
DI float gate_phase(const u16* x, const u16* gw, int g  ){
  const int tid=otid(), lane=tid&63, wid=tid>>6;
  const u32x4 w=*(const u32x4*)(gw+lane*8);
  float r=0.f;
#pragma unroll
  for(int i=0;i<8;++i){ const int tok=g*64+wid*8+i;
    const u32x4 v=*(const u32x4*)(x+tok*512+lane*8); float s=0.f;
#pragma unroll
    for(int e=0;e<4;++e) s+=bflo(v[e])*bflo(w[e])+bfhi(v[e])*bfhi(w[e]);
    s=xhalf_sum(red32(s)); r=(lane==i)?s:r; }
  return 1.f/(1.f+__expf(-r));
}

constexpr int XB(int t){ return (t%3)==0 ? 0 : (t%3)==1 ? 131072 : 65536; }
struct Front { const u16 *x,*ckv,*wq,*wuk,*wuv,*wo,*win,*wsp,*wout,*gw; };
DI void front_phase(bf16x8 (&q)[8], f32x4 (&za)[4][2], const Front& P, char* ws, srd_t sw, char* lds, u64& tag){
  const int tid=otid(), lane=tid&63, wid=rfl(tid>>6), grp=wid>>2, w4=wid&3;
  const int b=blockIdx.x, xcd=b&7, slot=b>>3, h=xcd*4+(slot>>3), xq=slot&7;
  const bool isz=(h&7)!=7; const int zc=h-(h>>3);
  const unsigned lds0=(unsigned)(size_t)lds;
  const unsigned va0=a_src(lane,0,1024), va1=a_src(lane,1,1024), vb8=b_src(lane,8192), vi=(lane>>2)*3584+(((lane&3)^((lane>>5)<<1))<<4), vbq=vb8^((w4&1)<<5);
  const srd_t sX=mksrd(P.x,2048*1024), sQ=mksrd(P.wq,512*8192), sI=mksrd(P.win,isz?512*3584:0);
  const unsigned qaL=((15-xq)*128+w4*32)*1024, qaS=(xq*128+w4*32)*1024, qb=w4*8*8192+h*256, zb=w4*16*3584+zc*64;
  const unsigned l4=lds0+w4*4096, l2=lds0+w4*2048, l1=lds0+w4*1024;
  auto q_piece=[&](int v,int k,int t) AI { const int st=(t&1)*65536;
    if(k<2) bdmai(l2,st+32768+v*8192+k*1024,vbq,sQ,qb,(v*32+k*4+t*64)*8192);
    else if(k==2) bdmai(l1,st+49152+v*4096,vi,sI,zb,v*1792+t*64*3584);
    else bdmai(l4,XB(t)+v*16384+(k-3)*1024,(k&1)?va0:va1,sX,v?qaS:qaL,(k-3)*8192+t*128); };
  u32x4 kvp[16];
  const int kh=wid&1, db=wid>>1;
  unsigned kvo=0;
  auto kv_store=[&](int n) AI { const int i=n>>2, u=(n>>1)&1, m=n&1;
    st16(sw,kvo+(m? db*3072 : 0),(i>>1)*32768+(m? 16384+u*2048+(i&1)*1024 : (2*(i&1)+u)*4096),kvp[n]); };
  {
    const srd_t sC=mksrd(P.ckv,2048*1024), sW=mksrd(wid<4?P.wuk:P.wuv,512*8192);
    const unsigned kva=(xq*256+wid*32)*1024, kvb=w4*16*8192+h*256, lk=lds0+wid*4096, vb9=vb8^32u;
    const unsigned qa8=grp?qaS:qaL, qb8=qb+grp*32*8192, zb8=zb+grp*1792, l28=l2+grp*8192, l18=l1+grp*4096;
    auto kv_piece=[&](int i,int t) AI { const int st=(t&1)*65536;
      if(i<4) bdmai(lk,st+i*1024,(i&1)?va1:va0,sC,kva,i*8192+t*128); else bdmai(lk,st+32768+(i-4)*1024,(i&2)?vb9:vb8,sW,kvb,((i-4)*4+t*64)*8192); };
    auto q_own=[&](int k) AI {
      if(k<2) bdmai(l28,32768+k*1024,vbq,sQ,qb8,k*4*8192); else if(k==2) bdmai(l18,49152,vi,sI,zb8,0); else bdmai(lk,XB((k-3)>>2)+((k-3)&3)*1024,(k&1)?va0:va1,sX,qa8,((k-3)&3)*8192+((k-3)>>2)*128); };
#pragma unroll
    for(int i=0;i<8;++i) kv_piece(i,0);
    ldsp cpt[2], wpt[2];
    { const int c=lane&15, g=lane>>4, q=c>>2, pp=c&3;
#pragma unroll
      for(int s=0;s<2;++s){ cpt[s]=(ldsp)lds+(kh*128+c)*128+(((4*s+g)^(c>>1))<<4); padd(cpt[s],0);
        wpt[s]=(ldsp)lds+32768+(8*g+q)*256+(((db*4+s*2+(pp>>1))^((q<<2)|((g&1)<<1)))<<4)+(pp&1)*8; padd(wpt[s],0); } }
    f32x4 acc[8][4];
    bf16x8 W[4][2], C[3][2];
#define LC(kb,s) LDF(cpt[s],(kb)*2048)
#define LW(cb,s) LDT(wpt[(cb)&1],((cb)>>1)*16384+(s)*8192)
    auto kvtile=[&](auto TT,int d,int nops,int span,auto op) AI { constexpr int t=TT, U0=t==0?8:0, U1=t==7?40:32;
      if(t==0){ W[0][0]=LW(0,0); C[0][0]=LC(0,0); W[0][1]=LW(0,1); C[0][1]=LC(0,1);
#pragma unroll
        for(int i=2;i<8;++i) W[i>>1][i&1]=LW(i>>1,i&1);
        C[1][0]=LC(1,0); C[1][1]=LC(1,1); }
      SBAR();
#pragma unroll
      for(int u=U0;u<U1;++u){ const int kb=((u>>2)+6)&7, cb=u&3, r=(8*t+(u>>2)+1)%3, r2=(r+2)%3;
        const bool Z= t==0 || (t==1 && u<8);
        if(Z){ if(cb<2) mm2<true>(acc[kb][cb],W[cb][0],C[r][0],W[cb][1],C[r][1]); else mm2<true>(acc[kb][cb],C[r][0],W[cb][0],C[r][1],W[cb][1]); }
        else { if(cb<2) mm2(acc[kb][cb],W[cb][0],C[r][0],W[cb][1],C[r][1]); else mm2(acc[kb][cb],C[r][0],W[cb][0],C[r][1],W[cb][1]); }
        if((cb==1||cb==2) && u<32) C[r2][cb-1]=LC(u>>2,cb-1);
        if(u>=4 && u<8){ W[cb][0]=LW(cb,0); W[cb][1]=LW(cb,1); }
        if(u==31 && d!=0){ padd(cpt[0],d); padd(cpt[1],d); padd(wpt[0],d); padd(wpt[1],d); }
#pragma unroll
        for(int i2=0;i2<12;++i2) if(i2<nops && (i2*span*8)/nops==u-U0) op(i2);
        SBAR(); } };
    const unsigned tw=b*8+wid;
    sfor<8>([&](auto T) AI { constexpr int t=T;
      if(t==0 || t>5) waitv_bar<0>(); else if(t<=2) waitv_bar<2>(); else waitv_bar<1>();
      if(t==0){ const unsigned lo=mb_get<252>(), hi=mb_get<253>(); tag=(((u64)rfl(hi)<<32)|rfl(lo))+1; SBAR(); }
      if constexpr(t<5) kvtile(T,(t&1)?-65536:65536,9,2,[&](int i) AI { if(i<8) kv_piece(i,t+1);
          else { if(t<2) touch_asm(sW, lane<12 ? (128+t*192)*8192+((b*4+w4)*12+lane)*128 : 0xF0000000u);
            touch_asm(t==0? sC : t==1? sX : t==2? mksrd(P.win,512*3584) : t==4? mksrd(P.wout,896*1024) : sQ, lane<(t==3?16:8) ? (tw*(t==3?16:8)+lane)*128 : 0xF0000000u); } });
      else if constexpr(t<7) kvtile(T,(t&1)?-65536:65536,8,2,[&](int i) AI { kv_piece(i,t+1); });
      else    kvtile(T,0,11,4,[&](int i) AI { q_own(i); });
    });
#undef LC
#undef LW
    MFENCE();
    kvo=WS_KV+(h*32+xq*4+kh*2)*32768+db*1024+(otid()&63)*16;
#pragma unroll
    for(int n=0;n<16;++n){ const int i=n>>2, u=(n>>1)&1, m=n&1;
      const f32x4 lo= m? acc[2*i][2+u] : acc[2*i+u][0], hi= m? acc[2*i+1][2+u] : acc[2*i+u][1];
      kvp[n]=(u32x4){cvtpk(lo[0],lo[1]),cvtpk(lo[2],lo[3]),cvtpk(hi[0],hi[1]),cvtpk(hi[2],hi[3])}; PIN(kvp[n]); }
  }
  auto rest=[&](auto GR) AI { constexpr int grp=GR;
#define W2(NA,NB) do{ if constexpr((grp?NB:NA)<63) waitv_bar<(grp?NB:NA)>(); else BARRIER(); }while(0)
    GS G; gs_init(G,lds,wid*32);
    unsigned fa=0;
    sfor<8>([&](auto T) AI { constexpr int t=T; constexpr int d=XB(t+1)-XB(t), db=(t&1)?-65536:65536;
      if constexpr(t==0) W2(4,4); else if constexpr(t==1) W2(8,6); else if constexpr(t==2) W2(8,63); else if constexpr(t==3) W2(12,63); else if constexpr(t==4) W2(10,8);
      else if constexpr(t==5) W2(63,8); else if constexpr(t==6) W2(0,8); else W2(63,0);
      if constexpr(t==6 && grp==0){ flag_set(sw,1,xcd*32+slot,tag); if(wid==1) code_touch<12>(slot); }
      auto ld=[&](int i) AI { q_piece(i&1,i>>1,i<6? t+1 : t+2); };
      if constexpr(grp==0){
        if constexpr(t<2) ktile<t>(G,d,db,14,4,ld);
        else if constexpr(t==2) ktile<t>(G,d,db,18,4,[&](int i) AI { if(i<14) ld(i); else kv_store(i-14); });
        else if constexpr(t<5) ktile<t>(G,d,db,6,4,[&](int i) AI { kv_store(4+6*(t-3)+i); });
        else if constexpr(t<7) ktile<t>(G,d,db,0,4,[&](int){});
        else ktile<t>(G,0,0,2,4,[&](int i) AI { if(i==1 && wid==0){ fa=4096+(xcd*32+(slot&24)+(otid()&7))*8; poll_issue(ws,fa); } });
      } else {
        if constexpr(t<2) ktile<t>(G,d,db,6,4,[&](int i) AI { kv_store(6*t+i); });
        else if constexpr(t==2) ktile<t>(G,d,db,4,2,[&](int i) AI { kv_store(12+i); });
        else if constexpr(t<6) ktile<t>(G,d,db,14,4,ld);
        else if constexpr(t==6) ktile<t>(G,d,db,6,2,ld);
        else ktile<t>(G,0,0,0,4,[&](int){});
      }
    });
    MFENCE();
    const float c=0.08838834764831845f*1.4426950408889634f;
#pragma unroll
    for(int j=0;j<8;++j){ q[j]=__builtin_bit_cast(bf16x8,pack2(G.acc[2*(j&3)][j>>2]*c,G.acc[2*(j&3)+1][j>>2]*c)); PIN(q[j]); }
#pragma unroll
    for(int j=0;j<8;++j) za[j>>1][j&1]=G.acc[8+(j>>1)][j&1];
    if constexpr(grp==0){ if(wid==0) poll_finish(ws,fa,true,tag); }
    BARRIER();
#undef W2
  };
  if(grp==0) rest(std::integral_constant<int,0>{}); else rest(std::integral_constant<int,1>{});
}

constexpr float ATT_HR = 12.f;
#define PFD 6
#define PRM 1
constexpr int AEX(int i){ return (i>=7&&i<=10)||i==13 ? 1 : 0; }
#define INL1 1
#define INL2 1
struct AttS { f32x4 o[8][2]; f32x4 s[4][2]; f32x4 negm[2]; bf16x8 q[8]; bf16x8 p[4]; bf16x8 f[12]; bf16x8 one; f32x4 l[2]; float m[2]; };
template<bool DK,bool DMA,int VOFF,int KOFF,bool Z=false> DI void pv_blk(AttS& S, ldsp kp, const char* ksrc, unsigned voff, unsigned kdst){
  SBAR();
#pragma unroll
  for(int n=0;n<16;++n){ const int db=n>>1, qb=n&1;
    mm2<Z>(S.o[db][qb],S.f[(2*db)%12],S.p[qb*2],S.f[(2*db+1)%12],S.p[qb*2+1]);
    if(n<2) mm2<Z>(S.l[n],S.one,S.p[n*2],S.one,S.p[n*2+1]);
    const int nn=n+PFD;
    if(nn<16) S.f[nn%12]=LDF(kp,VOFF+nn*1024); else if(DK) S.f[nn%12]=LDF(kp,KOFF+(nn-16)*1024);
    if constexpr(DMA){ if(n==0) setm0(kdst); if(n==2) dma1<0>(ksrc,voff); if(n==6) dma1<1024>(ksrc,voff); if(n==10) dma1<2048>(ksrc,voff); if(n==14) dma1<3072>(ksrc,voff); }
    SBAR();
  }
}
template<int KOFF,int KB0,int KB1,bool ZC=false> DI void qk_blk(AttS& S, ldsp kp){
  SBAR();
#pragma unroll
  for(int kb=KB0;kb<KB1;++kb)
#pragma unroll
    for(int qb=0;qb<2;++qb){
#pragma unroll
      for(int u=0;u<2;++u){ const int f0=(16+4*kb+2*u)%12, f1=(17+4*kb+2*u)%12;
        if(u) mm2(S.s[kb][qb],S.f[f0],S.q[qb*4+2],S.f[f1],S.q[qb*4+3]);
        else if(ZC) mm2<true>(S.s[kb][qb],S.f[f0],S.q[qb*4],S.f[f1],S.q[qb*4+1]);
        else mm2c(S.s[kb][qb],S.negm[qb],S.f[f0],S.q[qb*4],S.f[f1],S.q[qb*4+1]);
        const int nn=16+4*kb+2*qb+u+PFD; if(nn<32) S.f[nn%12]=LDF(kp,KOFF+(nn-16)*1024); SBAR(); }
    }
}
template<bool FIRST> DI void rescale(AttS& S){
#pragma unroll
  for(int qb=0;qb<2;++qb){
    float a=MX3(S.s[0][qb][0],S.s[0][qb][1],S.s[0][qb][2]), b=MX3(S.s[0][qb][3],S.s[1][qb][0],S.s[1][qb][1]);
    a=MX3(a,S.s[1][qb][2],S.s[1][qb][3]); b=MX3(b,S.s[2][qb][0],S.s[2][qb][1]); a=MX3(a,S.s[2][qb][2],S.s[2][qb][3]); b=MX3(b,S.s[3][qb][0],S.s[3][qb][1]); a=MX3(a,S.s[3][qb][2],S.s[3][qb][3]);
    const float mx=xhalf_max(x16_max(__builtin_fmaxf(a,b)));
    const float dl=__builtin_fmaxf(mx+ATT_HR, FIRST? -1e30f : 0.f);
    S.m[qb] += dl; PIN(S.m[qb]);
    if(!FIRST){
      float f=__builtin_amdgcn_exp2f(-dl); asm volatile("s_nop 1" : "+v"(f)); mul4(S.l[qb],f); PIN(S.l[qb]);
#pragma unroll
      for(int e=0;e<8;++e){ mul4(S.o[e][qb],f); PIN(S.o[e][qb]); }
    }
#pragma unroll
    for(int e=0;e<4;++e){ sub4(S.s[e][qb],dl); PIN(S.s[e][qb]); }
    const float nm=-S.m[qb];
#pragma unroll
    for(int e=0;e<4;++e) S.negm[qb][e]=fmov_s(nm);
    PIN(S.negm[qb]);
  }
}
template<bool DMA> DI unsigned expsum(AttS& S, const char* vsrc, unsigned voff){
  unsigned acc=0;
#pragma unroll
  for(int g=0;g<4;++g){
    const int qb=g>>1, ks=g&1;
    float p[8];
#pragma unroll
    for(int e=0;e<8;++e) p[e]=__builtin_amdgcn_exp2f(S.s[2*ks+(e>>2)][qb][e&3]);
    if(DMA){ if(g==0) dma1<0>(vsrc,voff); if(g==1) dma1<1024>(vsrc,voff); if(g==2) dma1<2048>(vsrc,voff); if(g==3) dma1<3072>(vsrc,voff); }
    u32x4 w;
#pragma unroll
    for(int e=0;e<4;++e) w[e]=cvtpk(p[2*e],p[2*e+1]);
    acc|=w[0]|w[1]; acc|=w[2]|w[3];
    S.p[g]=__builtin_bit_cast(bf16x8,w);
    PIN(S.p[g]); PIN(acc);
    SBAR();
  }
  return acc;
}
template<int K> DI void trimask(AttS& S, int thr, float ninf){
#pragma unroll
  for(int e=0;e<4;++e){ const bool mk=e>thr; S.s[K][0][e]=mk?ninf:S.s[K][0][e]; S.s[K+1][1][e]=mk?ninf:S.s[K+1][1][e]; S.s[K+1][0][e]=fmov_s(ninf); }
}
template<int K> DI void allmask(AttS& S, float ninf){
#pragma unroll
  for(int e=0;e<16;++e) S.s[K+(e>>3)][(e>>2)&1][e&3]=fmov_s(ninf);
}
DI void attn_phase(u32x4 (&cx)[8], const bf16x8 (&qin)[8], f32x4 (&za)[4][2], const Front& P, char* ws, srd_t sw, char* lds, u64 tag){
  const int tid=otid(), lane=tid&63;
  const int wid=rfl(tid>>6), grp=wid>>2, w4=wid&3;
  const int b=blockIdx.x, xcd=b&7, slot=b>>3, h=xcd*4+(slot>>3), x=slot&7;
  const int L=15-x, nS=grp? 2*x+2 : 100;
  const unsigned lds0=(unsigned)(size_t)lds;
  const unsigned kd0=lds0+grp*65536+w4*4096, kd1=kd0+16384, vd0=kd0+32768, vd1=kd0+49152;
  const char* ksrc=ws+WS_KV+(size_t)h*32*32768+w4*4096;
  const char* vsrc=ksrc+16384;
  unsigned voff=lane*16;
  ldsp kp=(ldsp)lds+grp*65536+lane*16;
  ldsw qst=(ldsw)lds+131072+w4*8192+lane*16;
  const unsigned d0= grp? (1u<<(nS-2))|(x<7? 1u<<15 : 0u) : (x==7? 1u<<16 : 0u), d1= grp? (1u<<(nS-1))|(1u<<16) : 0u;
  const unsigned mskA= w4==1? d0 : w4==3? d1 : 0u, mskB= w4==0? d0|d1 : w4==3? 0u : d1;
  const unsigned dead= w4<2? d1 : 0u;
  const unsigned evbits = mskA | mskB | (grp? 3u<<(nS-1) : 0u);
  const unsigned jump = (unsigned)(18-nS)*32768u;
  const bool isz=(h&7)!=7; const int zc=h-(h>>3), rb=(grp? x : L)*4+w4;
  dma4(ksrc,voff,kd0);
  AttS S;
#pragma unroll
  for(int d=0;d<8;++d) S.q[d]=qin[d];
  if(grp==0){
#pragma unroll
    for(int d=0;d<8;++d) *(LAS bf16x8*)(qst+d*1024)=S.q[d];
  }
  u32x4 zr[2]={}; float stt[4]={0.f,0.f,0.f,0.f};
  {
    u32x4 z1a={}, z1b={}; float stv=0.f;
#pragma unroll
    for(int j=0;j<2;++j){
#pragma unroll
      for(int k=0;k<4;++k){ f32x4& v=za[2*j+(k>>1)][k&1]; const f32x2 a=gelu2((f32x2){v[0],v[1]}), c=gelu2((f32x2){v[2],v[3]}); v=(f32x4){a[0],a[1],c[0],c[1]}; PIN(v); }
      if(j==0) dma4(vsrc,voff,vd0); else { voff+=32768; dma4(ksrc,voff,kd1); } }
    if(isz){
      z1a=pack2(za[0][0],za[1][0]); z1b=pack2(za[0][1],za[1][1]);
#pragma unroll
      for(int u=0;u<2;++u){ const u32x4 w=pack2(za[2+u][0],za[2+u][1]);
        auto r0=__builtin_amdgcn_permlane16_swap(w[0],w[2],false,false); auto r1=__builtin_amdgcn_permlane16_swap(w[1],w[3],false,false);
        zr[u]=(u32x4){r0[0],r1[0],r0[1],r1[1]}; }
      float y[8];
#pragma unroll
      for(int e=0;e<8;++e){ const float a=za[2][e>>2][e&3], c=za[3][e>>2][e&3]; y[e]=dpp_sel<0xE4,0xC>(dpp_add<0x128>(a+c),dpp_add<0x128>(a*a+c*c)); }
#pragma unroll
      for(int e=0;e<4;++e) y[e]=dpp_add<0x4E>(dpp_add<0xB1>(dpp_sel<0xE4,0xA>(dpp_add<0x141>(y[e]),dpp_add<0x141>(y[e+4]))));
      float v0=(lane&1)? y[1] : y[0], v1=(lane&1)? y[3] : y[2]; PIN(v0); PIN(v1);
      stv=(lane&2)? v1 : v0;
    }
    const int gg=((h>>4)? x : L)*2+((h>>3)&1);
    if(!isz) stv=gate_phase(P.x,P.gw,gg);
    const unsigned zo= isz? WS_Z1+(rb*56+zc)*1024 : 0xE0000000u;
    st16(sw,lane*16,zo,z1a); st16(sw,lane*16,zo+28672,z1b);
    unsigned o0=WS_ST+rb*8192+zc*256+(((lane>>2)&1)*32+(lane>>4)*8+((lane>>3)&1)*4+(lane&3))*4, o1=lane<8? WS_GATE+(gg*64+wid*8+lane)*4 : 0xE0000000u; PIN(o0); PIN(o1);
    st4(sw,isz? o0 : o1,0,__float_as_uint(stv));
  }
  const srd_t sT= wid<5 ? mksrd(P.wsp,2048*4096) : mksrd(P.wo,4096*1024);
  unsigned toff= lane<8 ? (wid<5 ? ((xcd>>1)*512+((slot*5+wid)>>1)*7)*4096u+(xcd&1)*2048+((slot*5+wid)&1)*1024+lane*128 : xcd*524288u+((slot*3+wid-5)*44+lane)*128u) : 0xE0000000u;
  const unsigned tstep= wid<5 ? 4096u : 1024u;

  S.m[0]=S.m[1]=0.f; S.one=__builtin_bit_cast(bf16x8,(u32x4){0x3f803f80u,0x3f803f80u,0x3f803f80u,0x3f803f80u}); PIN(S.one);
  waitv_bar<11>();
  if(grp==1) BARRIER_NW();
#pragma unroll
  for(int n=0;n<PFD;++n) S.f[(16+n)%12]=LDF(kp,n*1024);
  qk_blk<0,0,4,true>(S,kp);
  waitv_bar<7>();
#define SMSEG(i,PAR,FIRST0) do{ \
    const bool swi = !FIRST0 && !PAR && ((i)==nS); \
    _Pragma("unroll") for(int n=0;n<PFD;++n) S.f[n]=LDF(kp,32768+PAR*16384+n*1024); \
    if constexpr((i)==13) *(LAS f32x4*)((ldsw)lds+grp*65536+16384+w4*4096+(otid()&15)*16)=(f32x4){stt[0],stt[1],stt[2],stt[3]}; \
    setm0(PAR? vd0 : vd1); \
    SBAR(); \
    if(__builtin_expect((evbits>>(i))&1,INL1)){ \
      const int l_=otid()&63; int tri=(l_&15)-4*(l_>>4); float ninf=-__builtin_inff(); PIN(ninf); PIN(tri); \
      if(PAR && (i)==nS-1){ \
        _Pragma("unroll") for(int d=0;d<8;++d) S.q[d]=*(const LAS bf16x8*)((ldsp)lds+131072+w4*8192+l_*16+d*1024); \
      } \
      if((mskA>>(i))&1) trimask<2>(S,tri,ninf); \
      if((mskB>>(i))&1){ trimask<0>(S,tri,ninf); allmask<2>(S,ninf); } \
      if((dead>>(i))&1) allmask<0>(S,ninf); \
      if(swi) rescale<true>(S); \
    } \
    if(FIRST0) rescale<true>(S); \
    const unsigned pw=expsum<((i)<16)>(S,vsrc,voff); \
    if(PAR) voff+=32768u; else voff+=((i)==nS-2)? jump : 32768u; \
    PIN(voff); \
      \
    if constexpr((i)==3 || (i)==15){ if(wid==0) flag_set(sw,(i)==3? 5 : 2,x*32+h,tag); else { touch_asm(sT,toff); toff+=tstep; PIN(toff); } } \
    else if constexpr((i)==14 || (i)==6){ if(wid==7) code_touch<16>(slot); else { touch_asm(sT,toff); toff+=tstep; PIN(toff); } } \
    else if constexpr((i)==5) poll_issue(ws,5*4096+(x*32+(otid()&31))*8); \
    else if constexpr((i)==7 || (i)==8){ const int l_=otid()&63; const unsigned so=(isz? WS_ST+rb*8192 : 0xE0000000u)+l_*16; \
      if constexpr((i)==7){ if(isz) poll_check(ws,5*4096+(x*32+(l_&31))*8,(l_&7)!=7,tag); mb_ld16<236>(sw,so); mb_ld16<240>(sw,so+1024); } else { mb_ld16<244>(sw,so+2048); mb_ld16<248>(sw,so+3072); } } \
    else if constexpr((i)==9 || (i)==10){ const int l_=otid()&63; const unsigned so=(isz? WS_ST+rb*8192 : 0xE0000000u)+l_*16; constexpr int R=(i)==9? 236 : 244; \
      sfor<8>([&](auto K) AI { constexpr int k=K; stt[k&3]+=__uint_as_float(mb_get<R+k>()); PIN(stt[k&3]); }); \
      mb_ld16<R>(sw,so+((i)==9? 4096 : 6144)); mb_ld16<R+4>(sw,(i)==9? so+5120 : 0xE0000000u+l_); } \
    else if constexpr((i)==11){ sfor<8>([&](auto K) AI { constexpr int k=K; stt[k&3]+=__uint_as_float(mb_get<236+k>()); PIN(stt[k&3]); }); touch_asm(sT,toff); toff+=tstep; PIN(toff); } \
    else if constexpr((i)==12){ sfor<4>([&](auto K) AI { constexpr int k=K; stt[k]+=__uint_as_float(mb_get<244+k>()); PIN(stt[k]); }); ln_stats(stt); touch_asm(sT,toff); toff+=tstep; PIN(toff); } \
    else if constexpr((i)==13){ ln_apply(zr,(ldsp)lds+grp*65536+16384+w4*4096); const int l_=otid()&63; \
      const unsigned zo=(isz? WS_Z2+(zc*128+rb)*1024 : 0xE0000000u)+((l_>>4)&1)*512+(l_>>5)*256+(l_&15)*16; st16(sw,zo,0,zr[0]); st16(sw,zo,65536,zr[1]); } \
    else { touch_asm(sT,toff); toff+=tstep; PIN(toff); } \
    if(!FIRST0 && __builtin_expect(__builtin_amdgcn_ballot_w64((pw&0x40004000u)!=0)!=0,0)){ rescale<false>(S); expsum<false>(S,vsrc,voff); } \
    if(__builtin_expect(swi,INL2)){   \
      const unsigned cp=WS_AF+((h>>3)*4096+(x*4+w4)*64+(h&7)*4)*1024+(otid()&63)*16; \
      _Pragma("unroll") for(int qb=0;qb<2;++qb){ const float sc=__builtin_amdgcn_rcpf(S.l[qb][0]); \
        _Pragma("unroll") for(int k=0;k<4;++k){ st16(sw,cp,(qb*32+k)*1024,pack2(S.o[2*k][qb]*sc,S.o[2*k+1][qb]*sc)); \
          S.o[2*k][qb]=(f32x4){0.f,0.f,0.f,0.f}; S.o[2*k+1][qb]=(f32x4){0.f,0.f,0.f,0.f}; PIN(S.o[2*k][qb]); PIN(S.o[2*k+1][qb]); } \
        S.l[qb]=(f32x4){0.f,0.f,0.f,0.f}; PIN(S.l[qb]); } \
    } \
    if constexpr(FIRST0) WAITV(8); else if constexpr(PAR) asm volatile("s_waitcnt vmcnt(%0)" :: "i"(5+AEX(i)) : "memory"); \
    else asm volatile("s_cmp_eq_u32 %0, %1\n\ts_cbranch_scc1 1f\n\ts_waitcnt vmcnt(%2)\n1:\n\ts_waitcnt vmcnt(%3)" :: "s"(nS), "i"(i), "i"(5+AEX(i)), "i"(13+AEX(i)) : "memory", "scc"); \
    }while(0)
#define MFSEG(PAR,i) do{ \
      BARRIER_NW(); \
      __builtin_amdgcn_s_setprio(PRM); \
      pv_blk<true,((i)<16),32768+(PAR^1)*16384,PAR*16384,(i)==1>(S,kp,ksrc,voff,PAR? kd0 : kd1); \
      _Pragma("unroll") for(int k=0;k<8;++k) asm volatile("" : "=v"(S.s[k>>1][k&1]));       \
      if((!PAR && (i)<16) || __builtin_expect(!((dead>>(i))&1),1)){ qk_blk<PAR*16384,0,2>(S,kp); \
        if(__builtin_expect(!((mskB>>(i))&1),1)) qk_blk<PAR*16384,2,4>(S,kp); } \
      __builtin_amdgcn_s_setprio(0); \
      if constexpr(!PAR) waitv_bar<((i)<16?5:1)+AEX((i)-1)>(); \
      else asm volatile("s_cmp_eq_u32 %0, %1\n\ts_cbranch_scc1 1f\n\ts_waitcnt vmcnt(%2)\n1:\n\ts_waitcnt vmcnt(%3) lgkmcnt(0)\n\ts_barrier" :: "s"(nS), "i"((i)-1), "i"(5+AEX((i)-1)), "i"(13+AEX((i)-1)) : "memory", "scc"); \
      }while(0)
  SMSEG(0,0,true);
  sfor<8>([&](auto I) AI { constexpr int i=2*I+1;
    MFSEG(1,i);
    SMSEG(i,1,false);
    MFSEG(0,i+1);
    SMSEG(i+1,0,false);
  });
  BARRIER_NW();
#define lane lane2
  const int lane2=otid()&63;
  unsigned fa=0;
  if(wid==0){ const int c_=(slot%7)*4+(lane&3); fa=2*4096+(((lane>>2)&7)*32+c_+c_/7)*8; poll_issue(ws,fa); }
  if(!((dead>>16)&1)) pv_blk<false,false,32768,0>(S,kp,ksrc,voff,kd0);
  MFENCE();
  const float l0=S.l[0][0], l1=S.l[1][0];
  if(grp==0){
    ldsw mw=(ldsw)lds+w4*8192+lane*16;
    WAITV(0);
#pragma unroll
    for(int g=0;g<16;++g) *(LAS f32x4*)(mw+(g>>3)*131072+(g&7)*1024)=S.o[g&7][g>>3];
    if(wid==0 && slot<28) poll_finish(ws,fa,true,tag);
    *(LAS f32x4*)((ldsw)lds+122880+w4*1024+lane*16)=(f32x4){S.m[0],l0,S.m[1],l1};
    BARRIER();
  } else {
    const f32x4 ot=*(const LAS f32x4*)((ldsp)lds+122880+w4*1024+lane*16);
    ldsp mr=(ldsp)lds+w4*8192+lane*16;
#pragma unroll
    for(int qb=0;qb<2;++qb){
      const float mm=__builtin_fmaxf(S.m[qb],ot[2*qb]);
      const float fa_=__builtin_amdgcn_exp2f(S.m[qb]-mm), fb_=__builtin_amdgcn_exp2f(ot[2*qb]-mm);
      const float inv=__builtin_amdgcn_rcpf((qb?l1:l0)*fa_+ot[2*qb+1]*fb_);
      const float ca=fa_*inv, cb=fb_*inv;
#pragma unroll
      for(int k=0;k<4;++k){ const f32x4 y0=*(const LAS f32x4*)(mr+qb*131072+(2*k)*1024), y1=*(const LAS f32x4*)(mr+qb*131072+(2*k+1)*1024);
        cx[qb*4+k]=pack2(S.o[2*k][qb]*ca+y0*cb,S.o[2*k+1][qb]*ca+y1*cb); } }
    WAITV(0);
  }
#undef lane
#undef SMSEG
#undef MFSEG
}

constexpr int SLOT(int n){ return ((n+1)%5)*32768; }
DI unsigned ring_next(unsigned o){ o+=32768; return o==163840? 0 : o; }
DI void tail_loader(const Front& P, char* ws, srd_t sw, char* lds, u64 tag){
  const int tid=otid(), lane=tid&63, lw=rfl(tid>>6)&3;
  const int b=blockIdx.x, xcd=b&7, slot=b>>3;
  const int tb128=(xcd&1)*8+(slot>>2), nb128=slot&3, sl=xcd>>1;
  const unsigned lds0=(unsigned)(size_t)lds+lw*4096;
  const unsigned vl=lane*16, vb=b_src(lane,1024), vc=vb^32u;
  const srd_t sb0=mksrd(P.wo,4096*1024), sb1=mksrd(P.wout,896*1024);
  const unsigned abase=WS_AF+(sl*4096+(tb128*4+lw)*64)*1024, hbase=WS_H+((tb128*4+lw)*112+sl*7)*1024;
  auto ostage=[&](int m,unsigned st) AI { const unsigned ao=abase+m*2048, bo=(sl*1024+m*64+lw*16)*1024+nb128*256, d=lds0+st;
    bdmas(d,0,vl,sw,ao,0); bdmas(d,1024,vl,sw,ao,1024); bdmas(d,2048,vl,sw,ao,32768); bdmas(d,3072,vl,sw,ao,33792);
    bdmai(d,16384,vb,sb0,bo,0); bdmai(d,17408,vc,sb0,bo,4096); bdmai(d,18432,vb,sb0,bo,8192); bdmai(d,19456,vc,sb0,bo,12288); };
  auto hstage=[&](int m,unsigned st) AI { const unsigned ao=hbase+m*1024, bo=(sl*224+m*32+lw*8)*1024+nb128*256, d=lds0+st, db=d-lw*2048;
    bdmas(d,0,vl,sw,ao,0); bdmas(d,1024,vl,sw,ao,28672); bdmas(d,2048,vl,sw,ao,57344); bdmas(d,3072,vl,sw,ao,86016);
    bdmai(db,16384,vb,sb1,bo,0); bdmai(db,17408,vc,sb1,bo,4096); };
  const int xo=tb128<8? tb128 : 15-tb128;
  unsigned k0=3*4096+(xo*32+sl*8+lane)*8, k1=2*4096+(xo*32+((tb128<8? 2:0)+(lane&1))*8+7)*8; PIN(k0); PIN(k1);
  const unsigned fa3= lane<8 ? k0 : k1, fa4=4*4096+(tb128*16+(lane&15))*8;
  unsigned fill;
  if(slot<28){
    const int ts=(xcd>>1)*4+slot/7, kh=xcd&1, fb128=slot%7;
    const srd_t sa=mksrd(P.wsp,2048*4096);
    const unsigned va0=a_src(lane,0,4096), va1=a_src(lane,1,4096);
    const unsigned wbase=(ts*128+lw*32)*4096+kh*2048, zbase=WS_Z2+(fb128*4+lw)*131072+kh*32768;
    auto sstage=[&](int n,unsigned st) AI { const unsigned wo_=wbase+n*128, zo=zbase+n*2048, d=lds0+st;
      bdmai(d,0,va0,sa,wo_,0); bdmai(d,1024,va1,sa,wo_,32768); bdmai(d,2048,va0,sa,wo_,65536); bdmai(d,3072,va1,sa,wo_,98304);
      bdmas(d,16384,vl,sw,zo,0); bdmas(d,17408,vl,sw,zo,1024); bdmas(d,18432,vl,sw,zo,65536); bdmas(d,19456,vl,sw,zo,66560); };
    sstage(0,SLOT(0)); sstage(1,SLOT(1));
    waitv_barn<0>();
    sstage(2,SLOT(2)); sstage(3,SLOT(3)); sstage(4,SLOT(4));
    fill=SLOT(5);
#pragma nounroll
    for(int n=1;n<11;++n){ waitv_barn<16>(); sstage(n+4,fill); fill=ring_next(fill); }
    if(lw==0) poll_issue(ws,fa3);
    waitv_barn<16>(); sstage(15,SLOT(15));
    waitv_barn<16>();
    { const unsigned zo=WS_Z1+((ts*4+lw)*56+fb128*4)*1024, d=lds0+lw*4096+SLOT(16);
      sfor<8>([&](auto K) AI { constexpr int k=K; bdmas(d,k*1024,vl,sw,zo,(k>>2)*28672+(k&3)*1024); }); }
    WAITV(16); if(lw==0) poll_check(ws,fa3,lane<10,tag);
    fill=SLOT(17);
#pragma nounroll
    for(int m=0;m<4;++m){ waitv_barn<16>(); ostage(m,fill); fill=ring_next(fill); }
  } else {
    BARRIER_NW();
    if(lw==0){ poll_issue(ws,fa3); poll_finish(ws,fa3,lane<10,tag); }
    BARRIER_NW();
    fill=SLOT(17);
#pragma nounroll
    for(int m=0;m<4;++m){ ostage(m,fill); fill=ring_next(fill); }
    waitv_barn<24>();
  }
#pragma nounroll
  for(int m=4;m<14;++m){ waitv_barn<16>(); ostage(m,fill); fill=ring_next(fill); }
  if(lw==0) poll_issue(ws,fa4);
  waitv_barn<16>(); ostage(14,SLOT(31)); waitv_barn<16>(); ostage(15,SLOT(32));
  WAITV(16); if(lw==0) poll_check(ws,fa4,lane<14,tag);
  BARRIER_NW(); hstage(0,SLOT(33));
  fill=SLOT(34);
#pragma nounroll
  for(int m=1;m<7;++m){ waitv_barn<12>(); hstage(m,fill); fill=ring_next(fill); }
  waitv_barn<12>(); waitv_barn<6>(); waitv_barn<0>(); BARRIER_NW();
}
DI unsigned ptag(u64 tag,int e){ return ((unsigned)tag+e*0x155u)&1023u; }
DI void tail_compute(const u32x4 (&cx)[8], const Front& P, const u16* bsp, char* ws, srd_t sw, char* lds, u64 tag){
  const int tid=otid(), lane=tid&63, c16=lane&15, g4=lane>>4, wid=rfl(tid>>6)&3;
  const int b=blockIdx.x, xcd=b&7, slot=b>>3, h=xcd*4+(slot>>3), xq=slot&7;
  const int tb128=(xcd&1)*8+(slot>>2), nb128=slot&3, sl=xcd>>1;
  const int rh=wid&1, ch=wid>>1;
  const unsigned cp=WS_AF+((h>>3)*4096+((15-xq)*4+wid)*64+(h&7)*4)*1024+lane*16;
  const unsigned fl=lane*16;
  u32x4 hx[8];
  bf16x8 T[2][4][2], N[4][2];
  if(slot<28){
    const int ts=(xcd>>1)*4+slot/7, kh=xcd&1;
    ldsp tp[2];
#pragma unroll
    for(int s=0;s<2;++s){ const int row=rh*64+c16; tp[s]=(ldsp)lds+row*128+(((4*s+g4)^((row>>1)&7))<<4); padd(tp[s],SLOT(0)); }
    ldsp np=(ldsp)lds+16384+ch*8192+fl; padd(np,SLOT(0));
    f32x4 acc[4][4];
    BARRIER();
#define SN(nb,s) LDF(np,(nb)*2048+(s)*1024)
#pragma unroll
    for(int i=0;i<4;++i) N[i>>1][i&1]=SN(i>>1,i&1);
#pragma unroll
    for(int i=0;i<8;++i) T[0][i>>1][i&1]=LDF(tp[i&1],(i>>1)*2048);
    padd(tp[0],SLOT(1)-SLOT(0)); padd(tp[1],SLOT(1)-SLOT(0));
    sfor<16>([&](auto NN) AI { constexpr int n=NN, pb=n&1;
      if constexpr(n==8) WAITV(0);
      if constexpr(n>0) BARRIER_NW();
      SBAR();
#pragma unroll
      for(int u=0;u<16;++u){ const int nb=u>>2, tb=u&3;
        mm2<n==0>(acc[nb][tb],N[nb][0],T[pb][tb][0],N[nb][1],T[pb][tb][1]);
        if(tb<2){ if(nb<2 || n<15) N[(nb+2)&3][tb]=SN((nb+2)&3,tb); }
        else if(n<15) T[pb^1][nb][tb-2]=LDF(tp[tb-2],nb*2048);
        if(u==5) padd(np,SLOT(n+1)-SLOT(n));
        if(u==15){ padd(tp[0],SLOT(n+2)-SLOT(n+1)); padd(tp[1],SLOT(n+2)-SLOT(n+1)); }
        if(n<2 && tb==3) st16(sw,cp,(n*32+nb)*1024,cx[4*n+nb]);
        SBAR(); }
      if constexpr(n==3){ const srd_t sB=mksrd(bsp,kh?0:4096); sfor<4>([&](auto K) AI { constexpr int k=K; mb_ld2<234+k>(sB,(ts*128+rh*64+k*16+c16)*2); }); }
      if constexpr(n==8) flag_set<256>(sw,3,xq*32+h,tag);
    });
#undef SN
    BARRIER_NW();
    MFENCE();
    const unsigned bb[4]={mb_get<234>(),mb_get<235>(),mb_get<236>(),mb_get<237>()};
    ldsp z1p=(ldsp)lds+rh*16384+ch*2048+fl; padd(z1p,SLOT(16));
#pragma unroll
    for(int k=0;k<8;++k){ const int tb=k>>1, j=k&1; const float bv=bflo(bb[tb]);
      const u32x4 z1=*(const LAS u32x4*)(z1p+tb*4096+j*1024); u32x4 o;
#pragma unroll
      for(int e=0;e<4;++e) o[e]=cvtpk(bflo(z1[e])*(acc[2*j+(e>>1)][tb][(2*e)&3]+bv), bfhi(z1[e])*(acc[2*j+(e>>1)][tb][(2*e+1)&3]+bv));
      hx[k]=o; }
  } else {
#pragma unroll
    for(int j=0;j<8;++j){ st16(sw,cp,((j>>2)*32+(j&3))*1024,cx[j]); hx[j]=(u32x4){0,0,0,0}; }
    waitv_bar<0>(); flag_set<256>(sw,3,xq*32+h,tag);
    BARRIER_NW();
    BARRIER_NW();
  }
  const unsigned hp= slot<28 ? WS_H+((((xcd>>1)*4+slot/7)*4+rh*2)*112+(xcd&1)*28+(slot%7)*4+ch*2)*1024+fl : 0xE0000000u;
  ldsp tp=(ldsp)lds+rh*8192+fl; padd(tp,SLOT(17));
  ldsp np[4];
  { const int q=c16>>2, p=c16&3;
#pragma unroll
    for(int nb=0;nb<4;++nb){ np[nb]=(ldsp)lds+16384+(4*g4+q)*256+(((ch*8+nb*2+(p>>1))^((q<<2)|((g4&1)<<1)))<<4)+(p&1)*8; padd(np[nb],SLOT(17)); } }
  f32x4 acc[4][4], ach[4][4];
#define OT(tb,s) LDF(tp,(tb)*2048+(s)*1024)
#define ON(nb,s) LDT2((unsigned)(size_t)np[nb],(s)*8192,4096)
#pragma unroll
  for(int i=0;i<4;++i) N[i>>1][i&1]=ON(i>>1,i&1);
#pragma unroll
  for(int i=0;i<8;++i) T[0][i>>1][i&1]=OT(i>>1,i&1);
  padd(tp,SLOT(18)-SLOT(17)); padd(np[0],SLOT(18)-SLOT(17)); padd(np[1],SLOT(18)-SLOT(17));
  unsigned gt[4]={0,0,0,0};
  sfor<23>([&](auto M) AI { constexpr int m=M, pb=m&1;
    if constexpr(m==0) asm volatile("s_waitcnt lgkmcnt(0)" ::: "memory");
    if constexpr(m==7) WAITV(0);
    BARRIER_NW();
    SBAR();
    auto& A= m<16 ? acc : ach;
#pragma unroll
    for(int u=0;u<16;++u){ const int nb=u>>2, tb=u&3;
      mm2<m==0||m==16>(A[nb][tb],N[nb][0],T[pb][tb][0],N[nb][m<16?1:0],T[pb][tb][1]);
      const int nn=(nb+2)&3, st= nb<2? m : m+1;
      if(tb<2){ if(st<23 && (st<16 || tb==0)) N[nn][tb]=ON(nn,tb); if(tb==1) padd(np[nn],SLOT(18+st)-SLOT(17+st)); }
      else if(m<22) T[pb^1][nb][tb-2]=OT(nb,tb-2);
      if(u==15) padd(tp,SLOT(19+m)-SLOT(18+m));
      if(m<2 && tb==3) st16(sw,hp,((2*m+(nb>>1))*56+(nb&1))*1024,hx[4*m+nb]);
      SBAR(); }
    if constexpr(m==3) sfor<4>([&](auto K) AI { constexpr int k=K; mb_ld4<234+k>(sw,WS_GATE+(tb128*128+rh*64+k*16+c16)*4); });
    if constexpr(m==7){ if(slot<28) flag_set<256>(sw,4,((xcd>>1)*4+slot/7)*16+(xcd&1)*7+slot%7,tag); }
  });
#undef OT
#undef ON
  MFENCE();
  gt[0]=mb_get<234>(); gt[1]=mb_get<235>(); gt[2]=mb_get<236>(); gt[3]=mb_get<237>();
  const int tile=tb128*4+nb128;
#pragma unroll
  for(int i=0;i<2;++i){ const int o=rh*2+i;
    f32x4 v[8];
#pragma unroll
    for(int k=0;k<8;++k){ const int nb=k>>1, tb=2*i+(k&1); const float g=__uint_as_float(gt[tb]), og=1.f-g; v[k]=g*acc[nb][tb]+og*ach[nb][tb]; }
    if(o==sl){
#pragma unroll
      for(int k=0;k<8;++k) *(LAS f32x4*)((ldsw)lds+SLOT(38)+ch*8192+k*1024+lane*16)=v[k];
    } else { const unsigned po=WS_PART+((tile*4+o)*4+sl)*16384+ch*8192+lane*16;
#pragma unroll
      for(int k=0;k<8;++k){ u32x4 w=__builtin_bit_cast(u32x4,v[k]);
#pragma unroll
        for(int e=0;e<4;++e) w[e]=(w[e]&~1023u)|ptag(tag,e);
        st16(sw,po,k*1024,w); } } }
}
DI void red_phase(srd_t sw, u16* out, char* lds, int tb128, int nb128, int sl, u64 tag){
  const int tid=otid(), lane=tid&63, wid=rfl(tid>>6);
  const int tile=tb128*4+nb128;
  const unsigned ro=((lane>>4)&1)*1024+(lane>>5)*512+(lane&15)*16;
  u32x4 v[3][2];
#pragma nounroll
  for(unsigned spin=0;spin<(1u<<16);++spin){
    unsigned bad=0;
#pragma unroll
    for(int k=0;k<3;++k){ const int src=(sl+1+k)&3; const unsigned o=WS_PART+((tile*4+sl)*4+src)*16384+wid*2048;
      v[k][0]=ld16(sw,ro,o); v[k][1]=ld16(sw,ro,o+256); }
#pragma unroll
    for(int k=0;k<6;++k)
#pragma unroll
      for(int e=0;e<4;++e) bad|=v[k>>1][k&1][e]^ptag(tag,e);
    if(__builtin_amdgcn_ballot_w64((bad&1023u)!=0)==0) break;
    asm volatile("s_sleep 2" ::: "memory");
  }
#pragma unroll
  for(int k=0;k<6;++k)
#pragma unroll
    for(int e=0;e<4;++e) v[k>>1][k&1][e]=(v[k>>1][k&1][e]&~1023u)|512u;
  f32x4 a=*(const LAS f32x4*)((ldsp)lds+SLOT(38)+wid*2048+ro), b=*(const LAS f32x4*)((ldsp)lds+SLOT(38)+wid*2048+256+ro);
#pragma unroll
  for(int k=0;k<3;++k){ a+=__builtin_bit_cast(f32x4,v[k][0]); b+=__builtin_bit_cast(f32x4,v[k][1]); }
  const int row=tb128*128+sl*32+(lane&31), col=nb128*128+wid*16+(lane>>5)*8;
  *(u32x4*)(out+(size_t)row*512+col)=(u32x4){cvtpk(a[0],a[1]),cvtpk(a[2],a[3]),cvtpk(b[0],b[1]),cvtpk(b[2],b[3])};
}

constexpr int LDS_BYTES=163840;
__global__ __launch_bounds__(512,2) __attribute__((amdgpu_num_vgpr(116))) void k_mega(const u16* x,const u16* ckv,const u16* wq,const u16* wuk,const u16* wuv,const u16* wo,
     const u16* win,const u16* wsp,const u16* bsp,const u16* wout,const u16* gw,char* ws,u16* out){
  extern __shared__ __attribute__((aligned(16))) char lds[];
  const int b=blockIdx.x, xcd=b&7, slot=b>>3;
  const srd_t sw=mksrd(ws,WS_END);
  const Front P{x,ckv,wq,wuk,wuv,wo,win,wsp,wout,gw};
  asm volatile("" ::: "v255");
  { u64 pc; unsigned sz; asm volatile("s_getpc_b64 %0\nLcode_begin:\n\ts_mov_b32 %1, Lcode_end-Lcode_begin" : "=s"(pc), "=s"(sz));
    if(rfl(threadIdx.x>>6)==7) touch_asm(mksrd((const void*)pc,sz),(slot*64+(threadIdx.x&63))*128); }
  u64 tag=0; poll_issue(ws,0u);
  f32x4 za[4][2]; bf16x8 q[8];
  front_phase(q,za,P,ws,sw,lds,tag);
  u32x4 cx[8];
  attn_phase(cx,q,za,P,ws,sw,lds,tag);
  if(rfl(threadIdx.x>>8)==0) tail_loader(P,ws,sw,lds,tag); else tail_compute(cx,P,bsp,ws,sw,lds,tag);
  const int tb128=(xcd&1)*8+(slot>>2), nb128=slot&3, sl=xcd>>1;
  BARRIER();
  red_phase(sw,out,lds,tb128,nb128,sl,tag);
  if(b==0 && rfl(threadIdx.x>>6)==0){
    const int l_=threadIdx.x;
#pragma nounroll
    for(unsigned spin=0;spin<(1u<<16);++spin){ bool bad=false;
#pragma unroll
      for(int k=0;k<4;++k) bad|=__hip_atomic_load((gu64*)(ws+WS_FLG+4096+(k*64+l_)*8),__ATOMIC_RELAXED,__HIP_MEMORY_SCOPE_AGENT)!=tag;
      if(__builtin_amdgcn_ballot_w64(bad)==0) break;
      __builtin_amdgcn_s_sleep(1); }
    flag_set(sw,0,0,tag);
  }
  asm volatile("Lcode_end:");
}

extern "C" void kernel_launch(void* const* d_in, const int* in_sizes, int n_in, void* d_out, int out_size, void* d_ws, size_t ws_size, hipStream_t stream){
  (void)hipFuncSetAttribute((const void*)k_mega,hipFuncAttributeMaxDynamicSharedMemorySize,LDS_BYTES);
  k_mega<<<256,512,LDS_BYTES,stream>>>((const u16*)d_in[0],(const u16*)d_in[1],(const u16*)d_in[2],(const u16*)d_in[3],(const u16*)d_in[4],(const u16*)d_in[5],
     (const u16*)d_in[7],(const u16*)d_in[8],(const u16*)d_in[9],(const u16*)d_in[10],(const u16*)d_in[11],(char*)d_ws,(u16*)d_out);
}
```
